```python
import torch

def _softmax(x, axis=-1):
    return torch.softmax(x, dim=axis)
def _gelu(x):
    return torch.nn.functional.gelu(x, approximate="tanh")
def _silu(x):
    return torch.nn.functional.silu(x)
def _sigmoid(x):
    return torch.sigmoid(x)
def _layernorm(x, axis=-1, eps=1e-5):
    m = x.mean(dim=axis, keepdim=True)
    v = x.var(dim=axis, keepdim=True, unbiased=False)
    return (x - m) / torch.sqrt(v + eps)
def _scatter_add(tgt, idx, src, axis=0):
    return tgt.clone().index_add_(axis, idx, src)
def _segment_sum(src, seg, n):
    out = torch.zeros((n,) + src.shape[1:], dtype=src.dtype)
    return out.index_add_(0, seg, src)
def _pad(x, pw):
    flat = tuple(v for pair in reversed(pw) for v in pair)
    return torch.nn.functional.pad(x, flat)

kernel_name = 'gated_mix__grouped_kv_mqa__glu_gated_reglu__prompt'

def setup_inputs():

    _w = (lambda a: (lambda t: t.to(torch.bfloat16) if t.is_floating_point() else t)(torch.as_tensor(a)))
    def _A_setup():
        B, S, D, H = 1, 2048, 640, 32
        Dh = D // H
        return {
            "x": _w((lambda *s: torch.randn(*s, dtype=torch.bfloat16) * 0.05)(B, S, D)),
            "wq": _w((lambda *s: torch.randn(*s, dtype=torch.bfloat16) * 0.8)(D, H * Dh)),
            "wk": _w((lambda *s: torch.randn(*s, dtype=torch.bfloat16) * 0.8)(D, Dh)),
            "wv": _w((lambda *s: torch.randn(*s, dtype=torch.bfloat16) * 0.05)(D, Dh)),
            "wo": _w((lambda *s: torch.randn(*s, dtype=torch.bfloat16) * 0.05)(H * Dh, D)),
        }
    _A_in = _A_setup()

    def _B_setup():
        B, S, D, F = 1, 2048, 640, 1536
        return {
            "x": _w((lambda *s: torch.randn(*s, dtype=torch.bfloat16) * 0.05)(B, S, D)),
            "W_gate": _w((lambda *s: torch.randn(*s, dtype=torch.bfloat16) * 0.8)(D, F) * (2.0 / D) ** 0.5),
            "W_up": _w((lambda *s: torch.randn(*s, dtype=torch.bfloat16) * 0.8)(D, F) * (2.0 / D) ** 0.5),
            "W_down": _w((lambda *s: torch.randn(*s, dtype=torch.bfloat16) * 0.8)(F, D) * (1.0 / F) ** 0.5),
        }
    _B_in = _B_setup()

    return {
        "A_x": _A_in["x"],
        "A_wq": _A_in["wq"],
        "A_wk": _A_in["wk"],
        "A_wv": _A_in["wv"],
        "A_wo": _A_in["wo"],
        "B_x": _B_in["x"],
        "B_W_gate": _B_in["W_gate"],
        "B_W_up": _B_in["W_up"],
        "B_W_down": _B_in["W_down"],
        "gate_w": _w((lambda *s: torch.randn(*s, dtype=torch.bfloat16) * 0.05)(640) * 16.0),
    }

def reference(A_x, A_wq, A_wk, A_wv, A_wo, B_x, B_W_gate, B_W_up, B_W_down, gate_w):
    def _A_ref(x, wq, wk, wv, wo):
        B, S, D = x.shape
        H = 32
        Dh = D // H
        q = (lambda x, *d: x.permute(*d))((x @ wq).reshape(B, S, H, Dh), 0, 2, 1, 3)
        k = x @ wk
        v = x @ wv
        scores = torch.einsum("bhqd,bkd->bhqk", q, k) * (Dh ** -0.5)
        p = _softmax(scores, axis=-1)
        o = torch.einsum("bhqk,bkd->bhqd", p, v)
        return (lambda x, *d: x.permute(*d))(o, 0, 2, 1, 3).reshape(B, S, H * Dh) @ wo

    def _B_ref(x, W_gate, W_up, W_down):
        gate = torch.einsum("bsd,df->bsf", x, W_gate)
        up = torch.einsum("bsd,df->bsf", x, W_up)
        gate = gate * (lambda x, dt: x.to(dt))(gate > 0, gate.dtype)
        h = gate * up
        return torch.einsum("bsf,fd->bsd", h, W_down)

    x = A_x
    g = _sigmoid((x * gate_w).sum(-1, keepdims=True))
    a = _A_ref(x, A_wq, A_wk, A_wv, A_wo)
    b = _B_ref(x, B_W_gate, B_W_up, B_W_down)
    return g * a + (1 - g) * b
```

```cpp
#include <hip/hip_runtime.h>
#include <hip/hip_bf16.h>
#include <stdint.h>

typedef __attribute__((ext_vector_type(8))) short bf16x8;
typedef __attribute__((ext_vector_type(4))) short s16x4;
typedef __attribute__((ext_vector_type(16))) float f32x16;
typedef __attribute__((ext_vector_type(4))) float f32x4;
typedef __attribute__((ext_vector_type(4))) unsigned u32x4;
typedef __attribute__((ext_vector_type(2))) unsigned u32x2;
#define LAS __attribute__((address_space(3)))
#define GAS __attribute__((address_space(1)))
#define DI __device__ __forceinline__
#define MFMA(a, b, c) __builtin_amdgcn_mfma_f32_32x32x16_bf16((a), (b), (c), 0, 0, 0)

constexpr int S_ = 2048, D_ = 640, H_ = 32, DH_ = 20, F_ = 1536;
constexpr size_t WS_Q = 0;
constexpr size_t WS_KV = WS_Q + (size_t)S_ * D_ * 2;
constexpr size_t WS_H = WS_KV + 64 * 2560;
constexpr size_t WS_O = WS_H + (size_t)S_ * F_ * 2;
constexpr size_t WS_G = WS_O + (size_t)S_ * D_ * 2;
constexpr size_t WS_KMAX = WS_G + S_ * 4;

DI unsigned cvtpk(float lo, float hi) {
  typedef __attribute__((ext_vector_type(2))) float f32x2;
  typedef __attribute__((ext_vector_type(2))) __bf16 bf16x2;
  f32x2 v = {lo, hi};
  bf16x2 r = __builtin_convertvector(v, bf16x2);
  return __builtin_bit_cast(unsigned, r);
}
DI float bflo(unsigned u) { return __uint_as_float(u << 16); }
DI float bfhi(unsigned u) { return __uint_as_float(u & 0xffff0000u); }
DI void glds16(const void* g, LAS void* l) {
  unsigned sv; const unsigned base = __builtin_amdgcn_readfirstlane((unsigned)(size_t)l);
  asm volatile("s_mov_b32 %0, m0\n\ts_mov_b32 m0, %2\n\ts_nop 0\n\tglobal_load_lds_dwordx4 %1, off\n\ts_mov_b32 m0, %0"
               : "=&s"(sv) : "v"(g), "s"(base) : "memory");
}
DI bf16x8 lds_b128(LAS const char* p) { return *(LAS const bf16x8*)p; }
DI s16x4 lds_tr(LAS const char* p) {
  typedef short v4i16_t __attribute__((ext_vector_type(4)));
  return __builtin_bit_cast(s16x4, __builtin_amdgcn_ds_read_tr16_b64_v4i16((LAS v4i16_t*)p));
}
DI bf16x8 cat4(s16x4 a, s16x4 b) { return __builtin_shufflevector(a, b, 0, 1, 2, 3, 4, 5, 6, 7); }
#define STAMP(ws, kid, i)
#define RSTAMP(ws, kid, i)
#define WAIT_VM(N) asm volatile("s_waitcnt vmcnt(" #N ")" ::: "memory")
#define BAR() do { asm volatile("s_waitcnt lgkmcnt(0)" ::: "memory"); __builtin_amdgcn_s_barrier(); asm volatile("" ::: "memory"); } while (0)

constexpr int K1_STAGE = 49152, K1_XOFF = 32768, K1_LDS = 3 * K1_STAGE;

template <bool SWAPV>
DI void k1_compute(LAS const char* st, int xbase, int wb0, int wb1, f32x16 (&acc)[2][2]) {
#pragma unroll
  for (int ks = 0; ks < 4; ++ks) {
    bf16x8 xf[2], wf[2];
#pragma unroll
    for (int t = 0; t < 2; ++t) xf[t] = lds_b128(st + K1_XOFF + t * 4096 + (xbase ^ (ks << 5)));
#pragma unroll
    for (int r = 0; r < 2; ++r) wf[r] = cat4(lds_tr(st + ks * 8192 + r * 256 + wb0), lds_tr(st + ks * 8192 + r * 256 + wb1));
#pragma unroll
    for (int t = 0; t < 2; ++t) {
      acc[t][0] = MFMA(wf[0], xf[t], acc[t][0]);
      if (SWAPV) acc[t][1] = MFMA(xf[t], wf[1], acc[t][1]);
      else acc[t][1] = MFMA(wf[1], xf[t], acc[t][1]);
    }
  }
}

__global__ __launch_bounds__(512, 2) void k1_proj(const char* __restrict__ x, const char* __restrict__ wq, const char* __restrict__ wk,
                                                  const char* __restrict__ wv, const char* __restrict__ wg, const char* __restrict__ wu,
                                                  const char* __restrict__ gw, char* __restrict__ ws) {
  extern __shared__ __attribute__((aligned(16))) char smem_raw[];
  LAS char* smem = (LAS char*)smem_raw;
  const int tid = threadIdx.x, lane = tid & 63, w = __builtin_amdgcn_readfirstlane(tid >> 6);
  const int b = blockIdx.x;
  if (b >= 240) {
    const int tok0 = (b - 240) * 128 + w * 16, l16 = lane & 15, sub = lane >> 4;
    u32x4 gv[5];
#pragma unroll
    for (int i = 0; i < 5; ++i) gv[i] = *(const u32x4*)(gw + (l16 + 16 * i) * 16);
#pragma unroll
    for (int it = 0; it < 4; ++it) {
      const int tok = tok0 + it * 4 + sub;
      float s = 0.f;
#pragma unroll
      for (int i = 0; i < 5; ++i) {
        u32x4 xv = *(const u32x4*)(x + (size_t)tok * 1280 + (l16 + 16 * i) * 16);
#pragma unroll
        for (int e = 0; e < 4; ++e) { s += bflo(xv[e]) * bflo(gv[i][e]); s += bfhi(xv[e]) * bfhi(gv[i][e]); }
      }
      s += __shfl_xor(s, 1); s += __shfl_xor(s, 2); s += __shfl_xor(s, 4); s += __shfl_xor(s, 8);
      if (l16 == 0) ((float*)(ws + WS_G))[tok] = 1.f / (1.f + __expf(-s));
    }
    return;
  }
  const int L = (b & 7) * 30 + (b >> 3), half = L / 120, idx = L % 120, cg = idx >> 3, tb = half * 8 + (idx & 7);
  const char* wsrc[4]; int wstr[4];
  {
    const int slot = (lane & 15) >> 1, hf = lane & 1;
#pragma unroll
    for (int i = 0; i < 4; ++i) {
      const int nb = 4 * i + (lane >> 4), q = slot ^ (i >= 2 ? 4 : 0), k = 8 * w + q, nbl = nb & 7;
      if (cg < 12) { wsrc[i] = (i < 2 ? wg : wu) + (size_t)k * 3072 + (cg * 128 + nbl * 16 + hf * 8) * 2; wstr[i] = 64 * 3072; }
      else if (cg < 14) { wsrc[i] = wq + (size_t)k * 1280 + ((cg - 12) * 256 + (i >> 1) * 128 + nbl * 16 + hf * 8) * 2; wstr[i] = 64 * 1280; }
      else if ((i & 1) == 0) { wsrc[i] = wq + (size_t)k * 1280 + (512 + (i >> 1) * 64 + nbl * 16 + hf * 8) * 2; wstr[i] = 64 * 1280; }
      else { wsrc[i] = ((nbl & 1) ? wv : wk) + k * 40 + (i == 1 ? hf * 16 : 24); wstr[i] = 64 * 40; }
    }
  }
  const char* xsrc[2];
#pragma unroll
  for (int i = 0; i < 2; ++i) {
    const int row = 16 * w + 8 * i + (lane >> 3), ch = (lane & 7) ^ ((row >> 1) & 7);
    xsrc[i] = x + (size_t)(tb * 128 + row) * 1280 + ch * 16;
  }
  auto stage = [&](int buf) {
    LAS char* st = smem + buf * K1_STAGE;
#pragma unroll
    for (int i = 0; i < 4; ++i) { glds16(wsrc[i], st + (w * 4 + i) * 1024); wsrc[i] += wstr[i]; }
#pragma unroll
    for (int i = 0; i < 2; ++i) { glds16(xsrc[i], st + K1_XOFF + (w * 2 + i) * 1024); xsrc[i] += 128; }
  };
  const int tp = w & 1, rp = w >> 1, r32 = lane & 31, hh = lane >> 5;
  const int xrow = 64 * tp + r32;
  const int xbase = xrow * 128 + ((hh ^ ((xrow >> 1) & 7)) << 4);
  const int sel = (lane >> 4) & 1, q4 = (lane & 15) >> 2, p4 = lane & 3;
  const int wbase = hh * 4096 + (2 * rp + 8 * sel) * 256 + q4 * 32 + p4 * 8;
  const int wb0 = wbase + sel * 128, wb1 = wbase + (1 - sel) * 128;
  f32x16 acc[2][2];
#pragma unroll
  for (int t = 0; t < 2; ++t)
#pragma unroll
    for (int r = 0; r < 2; ++r)
#pragma unroll
      for (int i = 0; i < 16; ++i) acc[t][r][i] = 0.f;
  const bool kvwave = (cg == 14 && rp == 2);
  stage(0); stage(1);
  int buf = 0;
  for (int s = 0; s < 10; ++s) {
    if (s < 9) WAIT_VM(6); else WAIT_VM(0);
    BAR();
    if (s < 8) stage(buf == 0 ? 2 : buf - 1);
    LAS const char* st = smem + buf * K1_STAGE;
    if (kvwave) k1_compute<true>(st, xbase, wb0, wb1, acc);
    else k1_compute<false>(st, xbase, wb0, wb1, acc);
    buf = buf == 2 ? 0 : buf + 1;
  }
  if (cg < 12) {
#pragma unroll
    for (int t = 0; t < 2; ++t)
#pragma unroll
      for (int r = 0; r < 2; ++r) {
        float hv[8];
#pragma unroll
        for (int j = 0; j < 8; ++j) hv[j] = fmaxf(acc[t][r][j], 0.f) * acc[t][r][8 + j];
        u32x4 o = {cvtpk(hv[0], hv[1]), cvtpk(hv[2], hv[3]), cvtpk(hv[4], hv[5]), cvtpk(hv[6], hv[7])};
        const int tt = tb * 4 + tp * 2 + t, fs = cg * 8 + rp * 2 + r;
        *(u32x4*)(ws + WS_H + ((size_t)(tt * 96 + fs) * 64 + lane) * 16) = o;
      }
  } else if (cg < 14 || rp < 2) {
    const float c = 0.22360679774997896f * 1.4426950408889634f;
    const int cA0 = cg < 14 ? (cg - 12) * 256 : 512, cB0 = cg < 14 ? cA0 + 128 : 576;
#pragma unroll
    for (int t = 0; t < 2; ++t)
#pragma unroll
      for (int r = 0; r < 2; ++r)
#pragma unroll
        for (int g = 0; g < 4; ++g) {
          const int col = (g < 2 ? cA0 : cB0) + 16 * (rp * 2 + r) + 8 * (g & 1) + 4 * hh;
          const int tt = tb * 4 + tp * 2 + t;
          u32x2 o = {cvtpk(acc[t][r][4 * g] * c, acc[t][r][4 * g + 1] * c), cvtpk(acc[t][r][4 * g + 2] * c, acc[t][r][4 * g + 3] * c)};
          *(u32x2*)(ws + WS_Q + ((size_t)(tt * 160 + (col >> 2)) * 32 + r32) * 8) = o;
        }
  } else if (rp == 2) {
#pragma unroll
    for (int t = 0; t < 2; ++t) {
      const int tt = tb * 4 + tp * 2 + t;
      char* kv = ws + WS_KV + (size_t)tt * 2560;
      float ss = 0.f;
#pragma unroll
      for (int g = 0; g < 3; ++g) {
        u32x2 o = {cvtpk(acc[t][0][4 * g], acc[t][0][4 * g + 1]), cvtpk(acc[t][0][4 * g + 2], acc[t][0][4 * g + 3])};
        const bool valid = (g < 2) || (hh == 1);
        float q = bflo(o[0]) * bflo(o[0]) + bfhi(o[0]) * bfhi(o[0]) + bflo(o[1]) * bflo(o[1]) + bfhi(o[1]) * bfhi(o[1]);
        if (valid) ss += q;
        if (g < 2) *(u32x2*)(kv + g * 512 + r32 * 16 + hh * 8) = o;
        else if (hh == 1) *(u32x2*)(kv + 1024 + r32 * 8) = o;
      }
      ss += __shfl_xor(ss, 32);
#pragma unroll
      for (int m = 1; m < 32; m <<= 1) ss = fmaxf(ss, __shfl_xor(ss, m));
      if (lane == 0) ((float*)(ws + WS_KMAX))[tt] = ss;
      const int d = r32 < 16 ? r32 : r32 - 4;
      const bool vv = r32 < 16 || (r32 >= 20 && r32 < 24);
#pragma unroll
      for (int s = 0; s < 2; ++s) {
        u32x4 o = {cvtpk(acc[t][1][8 * s], acc[t][1][8 * s + 1]), cvtpk(acc[t][1][8 * s + 2], acc[t][1][8 * s + 3]),
                   cvtpk(acc[t][1][8 * s + 4], acc[t][1][8 * s + 5]), cvtpk(acc[t][1][8 * s + 6], acc[t][1][8 * s + 7])};
        if (vv) *(u32x4*)(kv + 1280 + (2 * s + hh) * 320 + d * 16) = o;
      }
    }
  }
}

constexpr int K2_SLOT = 10240, K2_ONES = 4 * K2_SLOT, K2_LDS = 5 * K2_SLOT;
#define SB() __builtin_amdgcn_sched_barrier(0)
#define EX2(x) __builtin_amdgcn_exp2f(x)

__global__ __launch_bounds__(512, 2) void k2_attn(char* __restrict__ ws) {
  extern __shared__ __attribute__((aligned(16))) char smem_raw[];
  LAS char* smem = (LAS char*)smem_raw;
  const int tid = threadIdx.x, lane = tid & 63, w = __builtin_amdgcn_readfirstlane(tid >> 6);
  const int b = blockIdx.x, r32 = lane & 31, hh = lane >> 5;
  const int tb = (b & 7) * 4 + ((b >> 3) >> 3), hg = (b >> 3) & 7;
  const int tt = tb * 2 + (w & 1), head = hg * 4 + (w >> 1);
  RSTAMP(ws, 1, 30); STAMP(ws, 1, 0);
  const char* kvsrc = ws + WS_KV + w * 1280 + lane * 16;
  auto stage = [&](int step) {
    LAS char* dst = smem + (step & 3) * K2_SLOT + w * 1280;
    glds16(kvsrc + (size_t)step * K2_SLOT, dst);
    if (lane < 16) glds16(kvsrc + (size_t)step * K2_SLOT + 1024, dst + 1024);
  };
  stage(0); stage(1); stage(2);
  if (tid < 16) {
    u32x4 ones = {0x3f803f80u, 0x3f803f80u, 0x3f803f80u, 0x3f803f80u};
    *(LAS u32x4*)(smem + K2_ONES + (tid >> 2) * 2560 + 1280 + (tid & 3) * 320 + 64) = ones;
  }
  const char* qp = ws + WS_Q + ((size_t)(tt * 160 + head * 5) * 32 + r32) * 8;
  u32x2 qa = *(const u32x2*)(qp + (2 * hh) * 256), qb = *(const u32x2*)(qp + (2 * hh + 1) * 256), qc = *(const u32x2*)(qp + 4 * 256);
  if (hh) { qc[0] = 0u; qc[1] = 0u; }
  const float gtok = ((const float*)(ws + WS_G))[tt * 32 + r32];
  float km = ((const float*)(ws + WS_KMAX))[lane];
#pragma unroll
  for (int m = 1; m < 64; m <<= 1) km = fmaxf(km, __shfl_xor(km, m));
  u32x4 q0u = {qa[0], qa[1], qb[0], qb[1]}, q1u = {qc[0], qc[1], 0u, 0u};
  float qq = 0.f;
#pragma unroll
  for (int e = 0; e < 4; ++e) qq += bflo(q0u[e]) * bflo(q0u[e]) + bfhi(q0u[e]) * bfhi(q0u[e]);
#pragma unroll
  for (int e = 0; e < 2; ++e) qq += bflo(q1u[e]) * bflo(q1u[e]) + bfhi(q1u[e]) * bfhi(q1u[e]);
  qq += __shfl_xor(qq, 32);
  const float bound = sqrtf(qq * km) * 1.0001f + 1e-6f;
  const bool fast = __all(bound <= 40.f);
  const bf16x8 q0 = __builtin_bit_cast(bf16x8, q0u), q1 = __builtin_bit_cast(bf16x8, q1u);
  const int kaddr = hh * 512 + r32 * 16, ktaddr = 1024 + r32 * 8;
  const int vrow = r32 < 20 ? r32 : (r32 < 28 ? 4 : 12);
  const bool vones = (r32 >= 20 && r32 < 28);
  const int vaddr = 1280 + hh * 320 + vrow * 16 + (vones ? K2_ONES : 0);
  const int vmask = vones ? 0 : -1;
  f32x16 o;
#pragma unroll
  for (int i = 0; i < 16; ++i) o[i] = 0.f;
  if (fast) {
    f32x16 nb, sa, sb;
#pragma unroll
    for (int i = 0; i < 16; ++i) nb[i] = -bound;
    asm volatile("" : "+v"(nb));
    bf16x8 k0, v0, v1; u32x4 k1 = {0u, 0u, 0u, 0u}, p0, p1 = {0u, 0u, 0u, 0u};
    WAIT_VM(2); BAR();
#define LDK(base, off) do { k0 = lds_b128(smem + (base) + kaddr + (off)); u32x2 kt_ = *(LAS const u32x2*)(smem + (base) + ktaddr + (off)); k1[0] = kt_[0]; k1[1] = kt_[1]; } while (0)
    STAMP(ws, 1, 1);
    LDK(0, 0);
    sa = MFMA(k0, q0, nb); sa = MFMA(__builtin_bit_cast(bf16x8, k1), q1, sa);
    LDK(0, 2560);
    v0 = lds_b128(smem + vaddr); v1 = v0;
#define TILE(SC, SN, KB, KO, V1B, V1O, V0B, V0O) do { \
    SN = MFMA(k0, q0, nb); SB(); \
    SC[0] = EX2(SC[0]); SC[1] = EX2(SC[1]); SC[2] = EX2(SC[2]); SC[3] = EX2(SC[3]); \
    p0[0] = cvtpk(SC[0], SC[1]); p0[1] = cvtpk(SC[2], SC[3]); SB(); \
    SN = MFMA(__builtin_bit_cast(bf16x8, k1), q1, SN); SB(); \
    LDK(KB, KO); SB(); \
    SC[4] = EX2(SC[4]); SC[5] = EX2(SC[5]); SC[6] = EX2(SC[6]); SC[7] = EX2(SC[7]); \
    p0[2] = cvtpk(SC[4], SC[5]); p0[3] = cvtpk(SC[6], SC[7]); SB(); \
    o = MFMA(v1, __builtin_bit_cast(bf16x8, p1), o); SB(); \
    v1 = lds_b128(smem + (V1B) + (V1O) + 640); SB(); \
    SC[8] = EX2(SC[8]); SC[9] = EX2(SC[9]); SC[10] = EX2(SC[10]); SC[11] = EX2(SC[11]); \
    p1[0] = cvtpk(SC[8], SC[9]); p1[1] = cvtpk(SC[10], SC[11]); SB(); \
    o = MFMA(v0, __builtin_bit_cast(bf16x8, p0), o); SB(); \
    v0 = lds_b128(smem + (V0B) + (V0O)); SB(); \
    SC[12] = EX2(SC[12]); SC[13] = EX2(SC[13]); SC[14] = EX2(SC[14]); SC[15] = EX2(SC[15]); \
    p1[2] = cvtpk(SC[12], SC[13]); p1[3] = cvtpk(SC[14], SC[15]); SB(); \
  } while (0)
    for (int step = 0; step < 16; ++step) {
      if (step > 0) {
        if (step < 14) WAIT_VM(2); else WAIT_VM(0);
        asm volatile("s_waitcnt lgkmcnt(1)\n\ts_barrier" ::: "memory");
      }
      if (step + 3 < 16) stage(step + 3);
      const int cur = (step & 3) * K2_SLOT, nxt = ((step + 1) & 3) * K2_SLOT;
      const int vc = vaddr + (cur & vmask), vn = vaddr + (nxt & vmask);
      __builtin_amdgcn_s_setprio(3);
      TILE(sa, sb, cur, 2 * 2560, vc, 0, vc, 2560);
      __builtin_amdgcn_s_setprio(2);
      TILE(sb, sa, cur, 3 * 2560, vc, 2560, vc, 2 * 2560);
      __builtin_amdgcn_s_setprio(1);
      TILE(sa, sb, nxt, 0, vc, 2 * 2560, vc, 3 * 2560);
      __builtin_amdgcn_s_setprio(0);
      TILE(sb, sa, nxt, 2560, vc, 3 * 2560, vn, 0);
    }
    o = MFMA(v1, __builtin_bit_cast(bf16x8, p1), o);
    STAMP(ws, 1, 2);
  } else {
    float m_run = -1e30f;
    f32x16 z;
#pragma unroll
    for (int i = 0; i < 16; ++i) z[i] = 0.f;
    WAIT_VM(2); BAR();
    for (int step = 0; step < 16; ++step) {
      if (step > 0) {
        if (step < 14) WAIT_VM(2); else WAIT_VM(0);
        BAR();
      }
      if (step + 3 < 16) stage(step + 3);
      const int cur = (step & 3) * K2_SLOT;
      const int vc = vaddr + (cur & vmask);
      for (int t = 0; t < 4; ++t) {
        bf16x8 kf = lds_b128(smem + cur + t * 2560 + kaddr);
        u32x2 kt = *(LAS const u32x2*)(smem + cur + t * 2560 + ktaddr);
        u32x4 ktu = {kt[0], kt[1], 0u, 0u};
        bf16x8 v0 = lds_b128(smem + vc + t * 2560), v1 = lds_b128(smem + vc + t * 2560 + 640);
        f32x16 s = MFMA(kf, q0, z);
        s = MFMA(__builtin_bit_cast(bf16x8, ktu), q1, s);
        float mx = s[0];
#pragma unroll
        for (int i = 1; i < 16; ++i) mx = fmaxf(mx, s[i]);
        mx = fmaxf(mx, __shfl_xor(mx, 32));
        const float mn = fmaxf(m_run, mx), al = EX2(m_run - mn);
        m_run = mn;
#pragma unroll
        for (int i = 0; i < 16; ++i) { o[i] *= al; s[i] = EX2(s[i] - mn); }
        u32x4 p0 = {cvtpk(s[0], s[1]), cvtpk(s[2], s[3]), cvtpk(s[4], s[5]), cvtpk(s[6], s[7])};
        u32x4 p1 = {cvtpk(s[8], s[9]), cvtpk(s[10], s[11]), cvtpk(s[12], s[13]), cvtpk(s[14], s[15])};
        o = MFMA(v0, __builtin_bit_cast(bf16x8, p0), o);
        o = MFMA(v1, __builtin_bit_cast(bf16x8, p1), o);
      }
    }
  }
  const float l = hh ? o[8] : o[12];
  const float sc = gtok / l;
#pragma unroll
  for (int g = 0; g < 3; ++g) {
    const int Qd = head * 5 + 2 * g + hh;
    u32x2 ov = {cvtpk(o[4 * g] * sc, o[4 * g + 1] * sc), cvtpk(o[4 * g + 2] * sc, o[4 * g + 3] * sc)};
    if (g < 2 || hh == 0)
      *(u32x2*)(ws + WS_O + ((size_t)(tt * 40 + (Qd >> 2)) * 64 + (Qd & 1) * 32 + r32) * 16 + ((Qd >> 1) & 1) * 8) = ov;
  }
}

constexpr int K3_STAGE = 36864, K3_WOFF = 16384, K3_LDS = 4 * K3_STAGE;

__global__ __launch_bounds__(768, 3) void k3_out(const char* __restrict__ wd, const char* __restrict__ wo, const char* __restrict__ ws,
                                                 char* __restrict__ out) {
  extern __shared__ __attribute__((aligned(16))) char smem_raw[];
  LAS char* smem = (LAS char*)smem_raw;
  const int tid = threadIdx.x, lane = tid & 63, w = __builtin_amdgcn_readfirstlane(tid >> 6);
  const int b = blockIdx.x, r32 = lane & 31, hh = lane >> 5;
  const int tb = (b & 7) * 4 + ((b >> 3) >> 3), cgp = (b >> 3) & 7;
  const char* src[3]; const char* src2[3]; int str[3], str2[3];
#pragma unroll
  for (int i = 0; i < 3; ++i) {
    const int p = w + 12 * i;
    if (p < 16) {
      const int kh = p >> 3, ksl = (p >> 1) & 3, t = p & 1, ttg = tb * 2 + t;
      src[i] = ws + WS_H + ((size_t)(ttg * 96 + kh * 68 + ksl) * 64 + lane) * 16; str[i] = 4096;
      src2[i] = kh ? ws + WS_O + ((size_t)(ttg * 40 + ksl) * 64 + lane) * 16 : src[i] + 7 * 4096; str2[i] = 4096;
    } else {
      const int pw = p - 16, kh = pw / 10, c = (pw % 10) * 64 + lane;
      const int ksl = c / 160, cc = c % 160, bi = cc >> 4, kb = bi / 5, nb = bi % 5, slot = (cc & 15) >> 1, hf = cc & 1;
      const int row = ksl * 16 + kb * 8 + (slot ^ ((nb & 1) << 2)), col = cgp * 80 + nb * 16 + hf * 8;
      src[i] = wd + (size_t)(kh * 68 * 16 + row) * 1280 + col * 2; str[i] = 64 * 1280;
      src2[i] = kh ? wo + (size_t)row * 1280 + col * 2 : src[i] + 7 * 64 * 1280; str2[i] = 64 * 1280;
    }
  }
  auto stage = [&](int s) {
    LAS char* st = smem + (s & 3) * K3_STAGE;
    if (s == 7) {
#pragma unroll
      for (int i = 0; i < 3; ++i) src[i] = src2[i];
    }
#pragma unroll
    for (int i = 0; i < 3; ++i) { glds16(src[i], st + (w + 12 * i) * 1024); src[i] += str[i]; }
  };
  const int kh = w / 6, tile = w % 6, t = tile & 1, rt = tile >> 1;
  const int sel = (lane >> 4) & 1, q4 = (lane & 15) >> 2, p4 = lane & 3;
  const int nb = rt < 2 ? 2 * rt + sel : 4 - sel;
  const int waddr = K3_WOFF + kh * 10240 + nb * 256 + (((4 * hh + q4) ^ ((nb & 1) << 2)) * 32) + p4 * 8;
  const int aaddr = kh * 8192 + t * 1024 + lane * 16;
  const float gtok = ((const float*)(ws + WS_G))[(tb * 2 + t) * 32 + r32];
  f32x16 acc;
#pragma unroll
  for (int i = 0; i < 16; ++i) acc[i] = 0.f;
  stage(0); stage(1); stage(2);
  for (int s = 0; s < 17; ++s) {
    if (s < 15) WAIT_VM(6); else if (s == 15) WAIT_VM(3); else WAIT_VM(0);
    BAR();
    if (s + 3 < 17) stage(s + 3);
    LAS const char* st = smem + (s & 3) * K3_STAGE;
    if (s == 7 && kh == 1) {
#pragma unroll
      for (int i = 0; i < 16; ++i) acc[i] *= (1.f - gtok);
    }
#pragma unroll
    for (int ks = 0; ks < 4; ++ks) {
      bf16x8 af = lds_b128(st + aaddr + ks * 2048);
      bf16x8 wf = cat4(lds_tr(st + waddr + ks * 2560), lds_tr(st + waddr + ks * 2560 + 1280));
      acc = MFMA(wf, af, acc);
    }
  }
  if (kh == 0) {
#pragma unroll
    for (int i = 0; i < 16; ++i) acc[i] *= (1.f - gtok);
  }
  BAR();
  {
    LAS float* red = (LAS float*)smem + kh * (64 * 84) + (t * 32 + r32) * 84 + rt * 32 + 4 * hh;
#pragma unroll
    for (int g = 0; g < 4; ++g) {
      f32x4 v = {acc[4 * g], acc[4 * g + 1], acc[4 * g + 2], acc[4 * g + 3]};
      if (rt < 2 || g < 2) *(LAS f32x4*)(red + 8 * g) = v;
    }
  }
  BAR();
  if (tid < 640) {
    const int tok = tid / 10, c8 = tid % 10;
    LAS const float* r0 = (LAS const float*)smem + tok * 84 + c8 * 8;
    f32x4 a0 = *(LAS const f32x4*)r0, a1 = *(LAS const f32x4*)(r0 + 4);
    f32x4 b0 = *(LAS const f32x4*)(r0 + 64 * 84), b1 = *(LAS const f32x4*)(r0 + 64 * 84 + 4);
    a0 += b0; a1 += b1;
    u32x4 ov = {cvtpk(a0[0], a0[1]), cvtpk(a0[2], a0[3]), cvtpk(a1[0], a1[1]), cvtpk(a1[2], a1[3])};
    *(u32x4*)(out + (size_t)(tb * 64 + tok) * 1280 + (cgp * 80 + c8 * 8) * 2) = ov;
  }
}

extern "C" void kernel_launch(void* const* d_in, const int* in_sizes, int n_in, void* d_out, int out_size, void* d_ws, size_t ws_size,
                              hipStream_t stream) {
  const char* x = (const char*)d_in[0];
  const char* wq = (const char*)d_in[1];
  const char* wk = (const char*)d_in[2];
  const char* wv = (const char*)d_in[3];
  const char* wo = (const char*)d_in[4];
  const char* wg = (const char*)d_in[6];
  const char* wu = (const char*)d_in[7];
  const char* wd = (const char*)d_in[8];
  const char* gw = (const char*)d_in[9];
  char* ws = (char*)d_ws;
  (void)hipFuncSetAttribute((const void*)k1_proj, hipFuncAttributeMaxDynamicSharedMemorySize, K1_LDS);
  (void)hipFuncSetAttribute((const void*)k2_attn, hipFuncAttributeMaxDynamicSharedMemorySize, K2_LDS);
  (void)hipFuncSetAttribute((const void*)k3_out, hipFuncAttributeMaxDynamicSharedMemorySize, K3_LDS);
  k1_proj<<<256, 512, K1_LDS, stream>>>(x, wq, wk, wv, wg, wu, gw, ws);
  k2_attn<<<256, 512, K2_LDS, stream>>>(ws);
  k3_out<<<256, 768, K3_LDS, stream>>>(wd, wo, ws, (char*)d_out);
}
```

```cpp
#include <hip/hip_runtime.h>
#include <hip/hip_bf16.h>
#include <stdint.h>

typedef __attribute__((ext_vector_type(8))) short bf16x8;
typedef __attribute__((ext_vector_type(4))) short s16x4;
typedef __attribute__((ext_vector_type(16))) float f32x16;
typedef __attribute__((ext_vector_type(4))) float f32x4;
typedef __attribute__((ext_vector_type(4))) unsigned u32x4;
typedef __attribute__((ext_vector_type(2))) unsigned u32x2;
#define LAS __attribute__((address_space(3)))
#define GAS __attribute__((address_space(1)))
#define DI __device__ __forceinline__
#define MFMA(a, b, c) __builtin_amdgcn_mfma_f32_32x32x16_bf16((a), (b), (c), 0, 0, 0)

constexpr int S_ = 2048, D_ = 640, H_ = 32, DH_ = 20, F_ = 1536;
constexpr size_t WS_Q = 0;
constexpr size_t WS_KV = WS_Q + (size_t)S_ * D_ * 2;
constexpr size_t WS_H = WS_KV + 64 * 2560;
constexpr size_t WS_O = WS_H + (size_t)S_ * F_ * 2;
constexpr size_t WS_G = WS_O + (size_t)S_ * D_ * 2;
constexpr size_t WS_CNT = WS_G + S_ * 4;
constexpr size_t WS_REQ = WS_CNT + 128 * 256;
constexpr size_t WS_ACK = WS_REQ + 2048;
constexpr size_t WS_OF = WS_ACK + 2048;
constexpr size_t WS_END = WS_OF + 16384;

DI unsigned cvtpk(float lo, float hi) {
  typedef __attribute__((ext_vector_type(2))) float f32x2;
  typedef __attribute__((ext_vector_type(2))) __bf16 bf16x2;
  f32x2 v = {lo, hi};
  bf16x2 r = __builtin_convertvector(v, bf16x2);
  return __builtin_bit_cast(unsigned, r);
}
DI float sq2(unsigned u, float c) {
  typedef __attribute__((ext_vector_type(2))) __bf16 bf16x2;
  const bf16x2 v = __builtin_bit_cast(bf16x2, u);
  return __builtin_amdgcn_fdot2_f32_bf16(v, v, c, false);
}
DI float bflo(unsigned u) { return __uint_as_float(u << 16); }
DI float bfhi(unsigned u) { return __uint_as_float(u & 0xffff0000u); }
DI void glds16s(const char* sbase, unsigned voff, unsigned ldsbase) {
  unsigned sv;
  asm volatile("s_mov_b32 %0, m0\n\ts_mov_b32 m0, %3\n\ts_nop 0\n\tglobal_load_lds_dwordx4 %1, %2\n\ts_mov_b32 m0, %0"
               : "=&s"(sv) : "v"(voff), "s"(sbase), "s"(ldsbase) : "memory");
}
DI void glds16c(const char* sbase, unsigned voff, unsigned ldsbase) {
  unsigned sv;
  asm volatile("s_mov_b32 %0, m0\n\ts_mov_b32 m0, %3\n\ts_nop 0\n\tglobal_load_lds_dwordx4 %1, %2 sc1\n\ts_mov_b32 m0, %0"
               : "=&s"(sv) : "v"(voff), "s"(sbase), "s"(ldsbase) : "memory");
}
DI bf16x8 lds_b128(LAS const char* p) { return *(LAS const bf16x8*)p; }
DI s16x4 lds_tr(LAS const char* p) {
  typedef short v4i16_t __attribute__((ext_vector_type(4)));
  return __builtin_bit_cast(s16x4, __builtin_amdgcn_ds_read_tr16_b64_v4i16((LAS v4i16_t*)p));
}
DI bf16x8 cat4(s16x4 a, s16x4 b) { return __builtin_shufflevector(a, b, 0, 1, 2, 3, 4, 5, 6, 7); }
#define WAIT_VM(N) asm volatile("s_waitcnt vmcnt(" #N ")" ::: "memory")
#define BAR() do { asm volatile("s_waitcnt lgkmcnt(0)" ::: "memory"); __builtin_amdgcn_s_barrier(); asm volatile("" ::: "memory"); } while (0)

#define SB() __builtin_amdgcn_sched_barrier(0)
#define EX2(x) __builtin_amdgcn_exp2f(x)
#define BC(x) __builtin_bit_cast(bf16x8, x)
#define MFMA8(a, b, c) __builtin_amdgcn_mfma_f32_32x32x8bf16_1k((a), (b), (c), 0, 0, 0)
#define LO4(x) __builtin_shufflevector((x), (x), 0, 1, 2, 3)
#define HI4(x) __builtin_shufflevector((x), (x), 4, 5, 6, 7)
typedef GAS unsigned gu32;
typedef GAS unsigned long long gu64;
#define RLX_AGENT __ATOMIC_RELAXED, __HIP_MEMORY_SCOPE_AGENT
constexpr int G0 = 3;
struct TrueT { static constexpr bool value = true; };
struct FalseT { static constexpr bool value = false; };
constexpr unsigned long long MIXC = 0x9E3779B97F4A7C15ull;
constexpr unsigned SPIN_CAP = 1u << 17;

constexpr int P1_STAGE = 49152, P1_XOFF = 32768, L_GW = 147456;
constexpr int KV_SLOT = 20480, L_KV = 49152, L_ONES = 151552, L_TOTAL = L_ONES + 10240;
constexpr int L_Q = 110592;
constexpr int RED_PITCH = 272, RED_W = 96 * RED_PITCH;
DI int gring(int gw) { return gw < 2 ? gw * 20480 : 110592 + (gw - 2) * 20480; }

DI void st8(char* p, unsigned lo, unsigned hi) {
  __hip_atomic_store((gu64*)p, (unsigned long long)lo | ((unsigned long long)hi << 32), RLX_AGENT);
}
DI void poll_ge(const char* word, unsigned need, int lane, int nl) {
  for (unsigned spins = 0; spins < SPIN_CAP; ++spins) {
    unsigned v = need;
    if (lane < nl) v = __hip_atomic_load((gu32*)word, RLX_AGENT);
    if (__all(v >= need)) break;
    __builtin_amdgcn_s_sleep(2);
  }
}
DI void lds_set(LAS char* p) { *(volatile LAS unsigned*)p = 1u; }
DI void lds_await(LAS char* p) {
  while (__builtin_amdgcn_readfirstlane(*(volatile LAS unsigned*)p) == 0u) __builtin_amdgcn_s_sleep(1);
  asm volatile("" ::: "memory");
}
DI void red_write(LAS char* red, int gw, int lane, const f32x16 (&acc)[3][2]) {
  LAS char* p = red + gw * RED_W + (lane & 31) * RED_PITCH + (lane >> 5) * 16;
#pragma unroll
  for (int t = 0; t < 3; ++t)
#pragma unroll
    for (int c = 0; c < 2; ++c)
#pragma unroll
      for (int g = 0; g < 4; ++g) {
        f32x4 v = {acc[t][c][4 * g], acc[t][c][4 * g + 1], acc[t][c][4 * g + 2], acc[t][c][4 * g + 3]};
        *(LAS f32x4*)(p + t * 32 * RED_PITCH + c * 128 + g * 32) = v;
      }
}
DI void red_read(LAS const char* red, int item, f32x4& a0, f32x4& a1) {
  LAS const char* p = red + (item >> 3) * RED_PITCH + (item & 7) * 32;
  a0 = *(LAS const f32x4*)p; a1 = *(LAS const f32x4*)(p + 16);
#pragma unroll
  for (int k = 1; k < 4; ++k) { a0 += *(LAS const f32x4*)(p + k * RED_W); a1 += *(LAS const f32x4*)(p + k * RED_W + 16); }
}

DI void p1_loader(unsigned lds0, const char* ab, int apstr, int asstr, int alds, int na, unsigned av0, unsigned av1,
                  const char* bb, int bpstr, int bsstr, int blds, int nb, unsigned bv0, unsigned bv1, const char* flag, unsigned& early) {
  unsigned st = lds0;
  const int n = na + nb;
#pragma unroll 1
  for (int s = -2; s < 10; ++s) {
    if (s >= 0) {
      if (s == 9) {
        WAIT_VM(0);
        if (flag) { __builtin_amdgcn_s_sleep(4); early = __builtin_amdgcn_readfirstlane(__hip_atomic_load((gu32*)flag, RLX_AGENT)); }
      } else if (n == 6) WAIT_VM(12); else if (n == 5) WAIT_VM(10); else WAIT_VM(8);
      BAR();
    }
    if (s < 8) {
      const char* g = ab; unsigned d = st + alds;
#pragma unroll 1
      for (int i = 0; i < na; ++i) { glds16s(g, av0, d); glds16s(g + apstr, av1, d + 1024); g += 2 * apstr; d += 2048; }
      g = bb; d = st + blds;
#pragma unroll 1
      for (int i = 0; i < nb; ++i) { glds16s(g, bv0, d); glds16s(g + bpstr, bv1, d + 1024); g += 2 * bpstr; d += 2048; }
      ab += asstr; bb += bsstr;
      st = st == lds0 + 2 * P1_STAGE ? lds0 : st + P1_STAGE;
    }
  }
}

DI void p1_loader_kv(unsigned lds0, const char* wk, const char* wv, unsigned l16, unsigned lo) {
  unsigned st = lds0;
#pragma unroll 1
  for (int s = -2; s < 10; ++s) {
    if (s >= 0) { if (s == 9) WAIT_VM(0); else WAIT_VM(6); BAR(); }
    if (s < 8) {
#pragma unroll
      for (int i = 0; i < 3; ++i) {
        glds16s(wk + i * 1024, i < 2 ? l16 : lo, st + 8192 + i * 1024);
        glds16s(wv + i * 1024, i < 2 ? l16 : lo, st + 16384 + i * 1024);
      }
      wk += 2560; wv += 2560;
      st = st == lds0 + 2 * P1_STAGE ? lds0 : st + P1_STAGE;
    }
  }
}

__global__ __launch_bounds__(768, 3) void fused_kernel(const char* __restrict__ x, const char* __restrict__ wq, const char* __restrict__ wk,
                                                       const char* __restrict__ wv, const char* __restrict__ wo, const char* __restrict__ wg,
                                                       const char* __restrict__ wu, const char* __restrict__ wd, const char* __restrict__ gwp,
                                                       char* __restrict__ ws, char* __restrict__ out) {
  extern __shared__ __attribute__((aligned(16))) char smem_raw[];
  LAS char* smem = (LAS char*)smem_raw;
  const unsigned lds0 = (unsigned)(size_t)smem;
  const int tid = threadIdx.x, lane = tid & 63, w = __builtin_amdgcn_readfirstlane(tid >> 6);
  const int b = blockIdx.x, r32 = lane & 31, hh = lane >> 5;
  const int xc = b & 7, bi = b >> 3;
  const int tbk = (xc >> 2) * 8 + (bi & 7), role = (xc & 3) + 4 * (bi >> 3);
  const int T = xc * 28 + bi;
  const bool active = bi < 28 && T < 220;
  const int tg = active ? T / 10 : 0, cg = active ? T % 10 : 0;
  const int tb = xc * 4 + (bi >> 3), hg = bi & 7;
  const unsigned l16 = lane * 16;
  const int sel = (lane >> 4) & 1, q4 = (lane & 15) >> 2, p4 = lane & 3;
  char* cnt = ws + WS_CNT;
  asm volatile("" :: "s"(x), "s"(wq), "s"(wk), "s"(wv), "s"(wo), "s"(wg), "s"(wu), "s"(wd), "s"(gwp), "s"(ws), "s"(out));
  if (__builtin_expect(w >= 8, 1)) {
    const int gw = w - 8;
    const int wrow = lane >> 3;
    unsigned early = 0u;
    f32x16 acc[3][2];
#pragma unroll
    for (int t = 0; t < 3; ++t) {
#pragma unroll
      for (int c = 0; c < 2; ++c)
#pragma unroll
        for (int i = 0; i < 16; ++i) acc[t][c][i] = 0.f;
    }
    {
      const char* xb = x + (size_t)tbk * 128 * 1280;
      const unsigned xoff0 = wrow * 1280 + (((lane & 7) ^ (wrow >> 1)) << 4), xoff1 = wrow * 1280 + (((lane & 7) ^ (4 + (wrow >> 1))) << 4);
      const int chs = (lane & 7) ^ (((wrow >> 1) & 1) << 2);
      const bool gu = role < 12, kvl = role == 15 && gw == 1;
      const unsigned lo = l16 < 512 ? l16 : 496u;
      const unsigned wv0 = gu ? wrow * 3072 + ((chs ^ ((gw >> 1) << 1)) << 4) : wrow * 1280 + chs * 16;
      const char* ab = gu ? (gw < 2 ? wg : wu) + (role * 128 + (gw & 1) * 64) * 2 : wq + ((role - 12) * 192 + gw * 64) * 2;
      const int na = gu ? 4 : role < 15 ? (gw < 3 ? 4 : 0) : (gw == 0 ? 4 : 0);
      const int nb = gu ? 2 : role < 15 ? (gw < 3 ? 1 : 5) : (gw < 2 ? 0 : 4);
      const int xp0 = gu ? gw * 4 : role < 15 ? gw * 2 : (gw & 1) * 8;
      if (!kvl)
        p1_loader(lds0, ab, gu ? 8 * 3072 : 8 * 1280, gu ? 64 * 3072 : 64 * 1280, gw * 8192, na, wv0, wv0, xb + xp0 * 10240, 10240, 128,
                  P1_XOFF + xp0 * 1024, nb, xoff0, xoff1, gu ? cnt + (gw == 1 ? 32 + (tb >> 1) : 64 + xc) * 256 : nullptr, early);
      else p1_loader_kv(lds0, wk, wv, l16, lo);
    }
    auto kvq = [&](int step, int q) {
      const char* kvb = ws + WS_KV + (size_t)((q >> 1) * 16 + (q & 1)) * 5120;
      const unsigned dst = lds0 + L_KV + (step % 3) * KV_SLOT + q * 5120;
#pragma unroll
      for (int i = 0; i < 5; ++i) glds16c(kvb + (size_t)step * 10240 + i * 1024, l16, dst + i * 1024);
    };
    auto kvstage = [&](int step) { kvq(step, gw); };
    __builtin_amdgcn_s_setprio(3);
    if (role >= 12) BAR();
    if (gw == 1) {
      if (early < 4u) poll_ge(cnt + (32 + (tb >> 1)) * 256, 4u, lane, 1);
      __builtin_amdgcn_fence(__ATOMIC_ACQUIRE, "wavefront");
#pragma unroll
      for (int i = 0; i < 10; ++i)
        glds16c(ws + WS_Q + (size_t)((tb * 2 + i / 5) * 40 + hg * 5 + i % 5) * 1024, l16, lds0 + L_Q + i * 1024);
      if (lane < 16) glds16c(ws + WS_G + tb * 256, l16, lds0 + L_Q + 10240);
    } else {
      if (gw == 0) {
        if (early < 16u) poll_ge(cnt + (64 + xc) * 256, 16u, lane, 1);
        __builtin_amdgcn_fence(__ATOMIC_ACQUIRE, "wavefront");
        lds_set(smem + L_ONES + 8);
      } else if (early < 16u) lds_await(smem + L_ONES + 8);
      kvstage(0); kvstage(1);
      if (gw >= 2) kvq(gw - 2, 1);
    }
    if (gw == 2 && lane == 0) glds16c(cnt + 72 * 256, l16, lds0 + L_Q + 10496);
#pragma unroll
    for (int t = 0; t < 3; ++t) asm volatile("" : "+v"(acc[t][0]), "+v"(acc[t][1]));
    WAIT_VM(0);
    BAR();
    kvstage(2);
    WAIT_VM(0);
    BAR();
    if (tid == 512 && role < 12) __hip_atomic_fetch_add((gu32*)(cnt + (48 + tbk) * 256), 1u, RLX_AGENT);
    kvstage(3);
    WAIT_VM(0);
#pragma unroll 1
    for (int step = 2; step < (active ? G0 - 1 : 8); ++step) { BAR(); if (step < 6) kvstage(step + 2); WAIT_VM(0); }
    if (!active) return;
    BAR();
    const unsigned woff = wrow * 1280 + (((lane & 7) ^ (((wrow >> 1) & 1) << 2)) << 4);
    const int wfrag = (4 * hh + q4) * 128 + ((sel ^ (((q4 >> 1) & 1) << 1)) << 5) + p4 * 8;
    const int gr = gring(gw);
    const char* hb[3]; int tts[3];
#pragma unroll
    for (int t = 0; t < 3; ++t) { int tt = tg * 3 + t; tt = tt < 64 ? tt : 63; tts[t] = tt; hb[t] = ws + WS_H + (size_t)(tt * 96 + gw * 24) * 1024; }
    const char* wb = wd + (size_t)(gw * 384) * 1280 + cg * 128;
    const char* wob = wo + (size_t)(gw * 160) * 1280 + cg * 128;
    const unsigned woff2 = ((wrow >> 2) * 20 + (wrow & 3)) * 1280 + (((lane & 7) ^ (((wrow >> 1) & 1) << 2)) << 4);
    auto gpiece = [&](int c, int q) {
      const unsigned dst = lds0 + gr + (c & 3) * 5120 + q * 1024;
      if (q < 3) glds16c(hb[q] + c * 1024, l16, dst);
      else glds16s(wb + (size_t)(c * 16 + (q - 3) * 8) * 1280, woff, dst);
    };
    auto wpiece = [&](int i) {
      const int ks = i >> 1, grp = ks >= 5, k5 = ks - 5 * grp;
      const bool tl = k5 == 4;
      glds16s(wob + (size_t)(tl ? 20 * (4 * grp + 2 * (i & 1)) + 16 : 20 * (4 * grp + k5) + 8 * (i & 1)) * 1280, tl ? woff2 : woff, lds0 + gr + i * 1024);
    };
    if (gw == 0) {
      poll_ge(cnt + ((lane & 1 ? 48 : 32) + ((lane & 2 ? tts[2] : tts[0]) >> 2)) * 256, lane & 1 ? 12u : 4u, lane, 4);
      __builtin_amdgcn_fence(__ATOMIC_ACQUIRE, "wavefront");
      lds_set(smem + L_ONES);
      kvstage(G0 + 1);
    } else { kvstage(G0 + 1); lds_await(smem + L_ONES); }
    if (lane < 24) glds16c(ws + WS_G, (lane < 8 ? tts[0] : lane < 16 ? tts[1] : tts[2]) * 128 + (lane & 7) * 16, lds0 + L_ONES + 128 + gw * 384);
#pragma unroll
    for (int c = 0; c < 3; ++c)
#pragma unroll
      for (int q = 0; q < 5; ++q) gpiece(c, q);
    WAIT_VM(15);
    auto gstep = [&](int c0, bool kv, auto last) {
#pragma unroll
      for (int u = 0; u < 4; ++u) {
        if (u < 3 && kv) WAIT_VM(15); else WAIT_VM(10);
        LAS const char* st = smem + gr + u * 5120;
        bf16x8 af[3], wf[2];
#pragma unroll
        for (int cc = 0; cc < 2; ++cc) wf[cc] = cat4(lds_tr(st + 3072 + (wfrag ^ (cc << 6))), lds_tr(st + 4096 + (wfrag ^ (cc << 6))));
#pragma unroll
        for (int t = 0; t < 3; ++t) af[t] = lds_b128(st + t * 1024 + l16);
        SB();
#pragma unroll
        for (int m = 0; m < 6; ++m) {
          acc[m >> 1][m & 1] = MFMA(wf[m & 1], af[m >> 1], acc[m >> 1][m & 1]); SB();
          if (m < 5) { if (decltype(last)::value && u > 0) wpiece((u - 1) * 5 + m); else gpiece(c0 + u + 3, m); }
          SB();
        }
      }
    };
#pragma unroll 1
    for (int step = G0; step < G0 + 5; ++step) {
      if (step < 8) BAR();
      if (step < 6) kvstage(step + 2);
      gstep(4 * (step - G0), step < 6, FalseT{});
    }
    gstep(20, false, TrueT{});
#pragma unroll
    for (int q = 0; q < 5; ++q) wpiece(15 + q);
#pragma unroll
    for (int t = 0; t < 3; ++t) {
      const float og = 1.f - *(LAS const float*)(smem + L_ONES + 128 + gw * 384 + t * 128 + r32 * 4);
#pragma unroll
      for (int c = 0; c < 2; ++c)
#pragma unroll
        for (int i = 0; i < 16; ++i) acc[t][c][i] *= og;
      asm volatile("" : "+v"(acc[t][0]), "+v"(acc[t][1]));
    }
    WAIT_VM(0);
    while (__builtin_amdgcn_readfirstlane(*(volatile LAS unsigned*)(smem + L_ONES + 12)) < 8u) __builtin_amdgcn_s_sleep(1);
    poll_ge(ws + WS_OF + (((lane & 1 ? tts[2] : tts[0]) >> 1) * 8 + 2 * gw + ((lane >> 1) & 1)) * 64, 1u, lane, 4);
    __builtin_amdgcn_fence(__ATOMIC_ACQUIRE, "wavefront");
    {
      auto rs = __builtin_amdgcn_make_buffer_rsrc((void*)(ws + WS_O), 0, S_ * D_ * 2, 0x00020000);
      LAS const char* st = smem + gr;
      LAS const char* so = smem + L_KV + gw * 15360 + l16;
      u32x4 af[4][3];
#pragma unroll
      for (int ks = 0; ks < 4; ++ks)
#pragma unroll
        for (int t = 0; t < 3; ++t) af[ks][t] = __builtin_amdgcn_raw_buffer_load_b128(rs, l16 + ks * 1024, (tts[t] * 40 + gw * 10) * 1024, 16);
      SB();
#pragma unroll
      for (int i = 0; i < 15; ++i)
        glds16c(ws + WS_O + (size_t)(tts[i % 3] * 40 + gw * 10 + 4 + i / 3) * 1024, l16, lds0 + L_KV + gw * 15360 + i * 1024);
      SB();
#pragma unroll
      for (int ks = 0; ks < 10; ++ks) {
        bf16x8 wf[2];
#pragma unroll
        for (int c = 0; c < 2; ++c) wf[c] = cat4(lds_tr(st + ks * 2048 + (wfrag ^ (c << 6))), lds_tr(st + ks * 2048 + 1024 + (wfrag ^ (c << 6))));
        if (ks == 4) WAIT_VM(3);
        if (ks >= 4 && ks < 9) {
#pragma unroll
          for (int t = 0; t < 3; ++t) af[1 + (ks & 1)][t] = *(LAS const u32x4*)(so + ((ks - 4) * 3 + t) * 1024);
        }
        const int ai = ks < 4 ? ks : (ks < 9 ? 1 + (ks & 1) : 0);
#pragma unroll
        for (int t = 0; t < 3; ++t)
#pragma unroll
          for (int c = 0; c < 2; ++c) acc[t][c] = MFMA(wf[c], BC(af[ai][t]), acc[t][c]);
        if (ks == 0) {
          SB();
#pragma unroll
          for (int t = 0; t < 3; ++t) af[0][t] = __builtin_amdgcn_raw_buffer_load_b128(rs, l16, (tts[t] * 40 + gw * 10 + 9) * 1024, 16);
          SB();
        }
      }
    }
    BAR();
    red_write(smem, gw, lane, acc);
  } else {
    const int kh = w >> 2, head = hg * 4 + (w & 3);
    unsigned long long nonce = 0ull;
    if (w == 0) {
      nonce = __hip_atomic_load((gu64*)(ws + WS_ACK + b * 8), RLX_AGENT) + MIXC;
      if (lane == 0) __hip_atomic_store((gu64*)(ws + WS_REQ + b * 8), nonce, RLX_AGENT);
    }
    if (tid >= 64 && tid < 82) {
      u32x4 ones = {0x3f803f80u, 0x3f803f80u, 0x3f803f80u, 0x3f803f80u};
      const int e = (tid - 64) % 9;
      LAS char* cp = smem + L_ONES + ((tid - 64) / 9) * 5120 + (e < 8 ? 2560 + e * 320 + 64 : 2048);
      *(LAS u32x4*)cp = ones;
      *(LAS u32x4*)(cp + 16) = u32x4{0u, 0u, 0u, 0u}; *(LAS u32x4*)(cp + 128) = u32x4{0u, 0u, 0u, 0u};
    }
    if (tid >= 128 && tid < 132) *(LAS u32x4*)(smem + L_ONES + (tid - 128) * 16) = u32x4{0u, 0u, 0u, 0u};
    auto ack_step = [&](int s) {
      if (w == 0) {
        if (s == 5 && lane == 0) glds16c(ws + WS_ACK + (b >> 1) * 16, 0u, lds0 + L_ONES + 64);
        if (s == 8) {
          WAIT_VM(0);
          unsigned long long ackv = *(volatile LAS unsigned long long*)(smem + L_ONES + 64 + (b & 1) * 8);
          for (unsigned spins = 0; ackv != nonce && spins < SPIN_CAP; ++spins) {
            __builtin_amdgcn_s_sleep(2);
            ackv = __hip_atomic_load((gu64*)(ws + WS_ACK + b * 8), RLX_AGENT);
          }
        }
      }
    };
    auto rs = __builtin_amdgcn_make_buffer_rsrc((void*)ws, 0, (int)WS_END, 0x00020000);
    u32x4 hp[2][2];
    if (role < 12) {
      const int tp = w & 1, rp = w >> 1;
      const int xrow = 64 * tp + r32;
      const int xbase = P1_XOFF + xrow * 128 + ((hh ^ ((xrow >> 1) & 7)) << 4);
      const int seg = (rp >> 1) + 2 * sel;
      const int wb0 = seg * 8192 + (8 * hh + q4) * 128 + ((((2 * rp) & 3) ^ (((q4 >> 1) & 1) << 1) ^ sel) << 5) + p4 * 8, wb1 = wb0 ^ 32;
      f32x16 acc[2][2];
#pragma unroll
      for (int t = 0; t < 2; ++t)
#pragma unroll
        for (int r = 0; r < 2; ++r)
#pragma unroll
          for (int i = 0; i < 16; ++i) acc[t][r][i] = 0.f;
      bf16x8 xf[2][2], wf[2][2];
      auto rd = [&](LAS const char* st, int ks, int f) {
#pragma unroll
        for (int t = 0; t < 2; ++t) xf[f][t] = lds_b128(st + t * 4096 + (xbase ^ (ks << 5)));
        wf[f][0] = cat4(lds_tr(st + ks * 2048 + wb0), lds_tr(st + ks * 2048 + 512 + wb0));
        wf[f][1] = cat4(lds_tr(st + ks * 2048 + wb1), lds_tr(st + ks * 2048 + 512 + wb1));
      };
      auto mm = [&](int f) {
#pragma unroll
        for (int t = 0; t < 2; ++t) { acc[t][0] = MFMA(wf[f][0], xf[f][t], acc[t][0]); acc[t][1] = MFMA(wf[f][1], xf[f][t], acc[t][1]); }
      };
#pragma unroll
      for (int t = 0; t < 2; ++t) { xf[1][t] = bf16x8{0, 0, 0, 0, 0, 0, 0, 0}; wf[1][t] = xf[1][t]; }
      int buf = 0;
#pragma unroll 1
      for (int s = 0; s < 10; ++s) {
        ack_step(s);
        __builtin_amdgcn_s_sleep(4);
        BAR();
        LAS const char* st = smem + buf * P1_STAGE;
        rd(st, 0, 0); SB(); mm(1); SB();
        rd(st, 1, 1); SB(); mm(0); SB();
        rd(st, 2, 0); SB(); mm(1); SB();
        rd(st, 3, 1); SB(); mm(0); SB();
        buf = buf == 2 ? 0 : buf + 1;
      }
      mm(1);
#pragma unroll
      for (int t = 0; t < 2; ++t)
#pragma unroll
        for (int r = 0; r < 2; ++r) {
          float hv[8];
#pragma unroll
          for (int j = 0; j < 8; ++j) hv[j] = fmaxf(acc[t][r][j], 0.f) * acc[t][r][8 + j];
          hp[t][r] = u32x4{cvtpk(hv[0], hv[1]), cvtpk(hv[2], hv[3]), cvtpk(hv[4], hv[5]), cvtpk(hv[6], hv[7])};
        }
      asm volatile("" : "+v"(hp[0][0]), "+v"(hp[0][1]), "+v"(hp[1][0]), "+v"(hp[1][1]));
    } else {
      const int tl = w & 3, rg = w >> 2, tt = tbk * 4 + tl;
      const int xrow = 32 * tl + r32;
      const int xbase = P1_XOFF + xrow * 128 + ((hh ^ ((xrow >> 1) & 7)) << 4);
      const int wl = (8 * hh + q4) * 128 + ((sel ^ (((q4 >> 1) & 1) << 1)) << 5) + p4 * 8;
      const int wkv = (8 * hh + q4) * 40 + sel * 32 + p4 * 8;
      if (role == 15 && rg == 1) {
        if (tid >= 256 && tid < 336) *(LAS u32x4*)(smem + L_GW + (tid - 256) * 16) = *(const u32x4*)(gwp + (tid - 256) * 16);
        const bool leader = (tbk == 0 && w == 7);
        unsigned long long ack0[4], rq[4]; bool served[4] = {false, false, false, false};
        if (leader) {
          __hip_atomic_store((gu32*)(cnt + lane * 256), 0u, RLX_AGENT);
          __hip_atomic_store((gu32*)(cnt + (64 + lane) * 256), 0u, RLX_AGENT);
#pragma unroll
          for (int i = 0; i < 4; ++i) __hip_atomic_store((gu32*)(ws + WS_OF + (lane + 64 * i) * 64), 0u, RLX_AGENT);
#pragma unroll
          for (int i = 0; i < 4; ++i) ack0[i] = __hip_atomic_load((gu64*)(ws + WS_ACK + (lane + 64 * i) * 8), RLX_AGENT);
        }
        auto rq_load = [&]() {
#pragma unroll
          for (int i = 0; i < 4; ++i) rq[i] = __hip_atomic_load((gu64*)(ws + WS_REQ + (lane + 64 * i) * 8), RLX_AGENT);
        };
        auto serve = [&]() -> bool {
          asm volatile("s_waitcnt vmcnt(0)" : "+v"(rq[0]), "+v"(rq[1]), "+v"(rq[2]), "+v"(rq[3]), "+v"(ack0[0]), "+v"(ack0[1]), "+v"(ack0[2]), "+v"(ack0[3]) :: "memory");
          bool all = true;
#pragma unroll
          for (int i = 0; i < 4; ++i) {
            if (!served[i] && rq[i] == ack0[i] + MIXC) { __hip_atomic_store((gu64*)(ws + WS_ACK + (lane + 64 * i) * 8), rq[i], RLX_AGENT); served[i] = true; }
            all = all && served[i];
          }
          return __all(all);
        };
        f32x16 acc;
#pragma unroll
        for (int i = 0; i < 16; ++i) acc[i] = 0.f;
        bool done = !leader;
        int buf = 0;
#pragma unroll 1
        for (int s = 0; s < 10; ++s) {
          if (!done && (s & 1) == 0) { if (s >= 2) done = serve(); if (!done) rq_load(); }
          if (s == 7) { for (unsigned spins = 0; !done && spins < SPIN_CAP; ++spins) { done = serve(); if (!done) rq_load(); } }
          BAR();
          LAS const char* st = smem + buf * P1_STAGE;
#pragma unroll
          for (int ks = 0; ks < 4; ++ks) {
            bf16x8 xf = lds_b128(st + (xbase ^ (ks << 5)));
            bf16x8 wf = cat4(lds_tr(st + 2 * 8192 + ks * 640 + wkv), lds_tr(st + 2 * 8192 + ks * 640 + 160 + wkv));
            bf16x8 gf = lds_b128(smem + L_GW + (s * 64 + ks * 16 + hh * 8) * 2);
            if (r32 >= 24) wf = gf;
            acc = MFMA(xf, wf, acc);
          }
          buf = buf == 2 ? 0 : buf + 1;
        }
        char* kv = ws + WS_KV + (size_t)(tt >> 1) * 5120 + 2560 + (tt & 1) * 1280;
        const int d = r32;
        if (r32 < 20) {
#pragma unroll
          for (int s = 0; s < 2; ++s) {
            st8(kv + (2 * s + hh) * 320 + d * 16, cvtpk(acc[8 * s], acc[8 * s + 1]), cvtpk(acc[8 * s + 2], acc[8 * s + 3]));
            st8(kv + (2 * s + hh) * 320 + d * 16 + 8, cvtpk(acc[8 * s + 4], acc[8 * s + 5]), cvtpk(acc[8 * s + 6], acc[8 * s + 7]));
          }
        }
        if (r32 == 24) {
#pragma unroll
          for (int i = 0; i < 16; i += 2) {
            const float g0 = 1.f / (1.f + __expf(-acc[i])), g1 = 1.f / (1.f + __expf(-acc[i + 1]));
            st8(ws + WS_G + (tt * 32 + (i & 3) + 8 * (i >> 2) + 4 * hh) * 4, __float_as_uint(g0), __float_as_uint(g1));
          }
        }
      } else {
        int wb[3];
#pragma unroll
        for (int r = 0; r < 3; ++r) { const int rt = 3 * rg + r; wb[r] = (rt >> 1) * 8192 + (wl ^ ((rt & 1) << 6)); }
        const int ks2 = role == 15 ? 640 : 2048, t2 = role == 15 ? 160 : 512;
        if (role == 15) wb[2] = 8192 + wkv;
        f32x16 acc[3];
#pragma unroll
        for (int r = 0; r < 3; ++r)
#pragma unroll
          for (int i = 0; i < 16; ++i) acc[r][i] = 0.f;
        bf16x8 xf[2], wf[2][3];
        auto rd = [&](LAS const char* st, int ks, int f) {
          xf[f] = lds_b128(st + (xbase ^ (ks << 5)));
#pragma unroll
          for (int r = 0; r < 2; ++r) wf[f][r] = cat4(lds_tr(st + ks * 2048 + wb[r]), lds_tr(st + ks * 2048 + 512 + wb[r]));
          wf[f][2] = cat4(lds_tr(st + ks * ks2 + wb[2]), lds_tr(st + ks * ks2 + t2 + wb[2]));
        };
        auto mm = [&](int f) {
#pragma unroll
          for (int r = 0; r < 3; ++r) acc[r] = MFMA(wf[f][r], xf[f], acc[r]);
        };
        xf[1] = bf16x8{0, 0, 0, 0, 0, 0, 0, 0}; wf[1][0] = xf[1]; wf[1][1] = xf[1]; wf[1][2] = xf[1];
        int buf = 0;
#pragma unroll 1
        for (int s = 0; s < 10; ++s) {
          ack_step(s);
          BAR();
          LAS const char* st = smem + buf * P1_STAGE;
          rd(st, 0, 0); SB(); mm(1); SB();
          rd(st, 1, 1); SB(); mm(0); SB();
          rd(st, 2, 0); SB(); mm(1); SB();
          rd(st, 3, 1); SB(); mm(0); SB();
          buf = buf == 2 ? 0 : buf + 1;
        }
        mm(1);
        const float c = 0.22360679774997896f * 1.4426950408889634f;
#pragma unroll
        for (int r = 2; r >= 0; --r) {
          if (role < 15 || r < 2) {
#pragma unroll
            for (int i = 0; i < 2; ++i) {
              u32x4 o = {cvtpk(acc[r][8 * i] * c, acc[r][8 * i + 1] * c), cvtpk(acc[r][8 * i + 2] * c, acc[r][8 * i + 3] * c),
                         cvtpk(acc[r][8 * i + 4] * c, acc[r][8 * i + 5] * c), cvtpk(acc[r][8 * i + 6] * c, acc[r][8 * i + 7] * c)};
              __builtin_amdgcn_raw_buffer_store_b128(o, rs, l16 + (unsigned)WS_Q + (tt * 40 + (role - 12) * 12 + (3 * rg + r) * 2 + i) * 1024, 0, 16);
            }
          } else {
            char* kv = ws + WS_KV + (size_t)(tt >> 1) * 5120;
            float ss = 0.f;
#pragma unroll
            for (int g = 0; g < 3; ++g) {
              const unsigned o0 = cvtpk(acc[r][4 * g], acc[r][4 * g + 1]), o1 = cvtpk(acc[r][4 * g + 2], acc[r][4 * g + 3]);
              const float q = bflo(o0) * bflo(o0) + bfhi(o0) * bfhi(o0) + bflo(o1) * bflo(o1) + bfhi(o1) * bfhi(o1);
              if (g < 2 || hh == 0) ss += q;
              if (g < 2) st8(kv + (tt & 1) * 1024 + g * 512 + r32 * 16 + hh * 8, o0, o1);
              else if (hh == 0) st8(kv + 2048 + r32 * 16 + (tt & 1) * 8, o0, o1);
            }
            ss += __shfl_xor(ss, 32);
#pragma unroll
            for (int m = 1; m < 32; m <<= 1) ss = fmaxf(ss, __shfl_xor(ss, m));
            if (lane == 0) __hip_atomic_fetch_max((gu32*)(cnt + 72 * 256), __float_as_uint(ss), RLX_AGENT);
          }
        }
      }
      WAIT_VM(0);
      BAR();
      if (tid == 0) __hip_atomic_fetch_add((gu32*)(cnt + (32 + tbk) * 256), 1u, RLX_AGENT);
      if (role == 15 && tid >= 64 && tid < 72) __hip_atomic_fetch_add((gu32*)(cnt + tid * 256), 1u, RLX_AGENT);
    }
    auto hstore = [&]() {
#pragma unroll
      for (int t = 0; t < 2; ++t)
#pragma unroll
        for (int r = 0; r < 2; ++r)
          __builtin_amdgcn_raw_buffer_store_b128(hp[t][r], rs, l16 + (unsigned)WS_H + ((tbk * 4 + (w & 1) * 2 + t) * 96 + role * 8 + (w >> 1) * 2 + r) * 1024, 0, 16);
    };
    const int vrow = r32 < 20 ? r32 : (r32 == 20 ? 4 : (r32 < 28 ? 5 : 12));
    const bool vones = r32 >= 20;
    int kaddr = L_KV + kh * 10240 + hh * 512 + r32 * 16;
    int taddr = hh ? L_ONES + 2048 : L_KV + kh * 10240 + 2048 + r32 * 16; const int tmask = hh ? 0 : -1;
    int vaddr = 2560 + hh * 320 + vrow * 16 + (vones ? L_ONES : L_KV + kh * 10240); const int vmask = vones ? 0 : -1;
    int qad[3];
#pragma unroll
    for (int i = 0; i < 3; ++i) {
      const int q = (w & 3) * 5 + (i < 2 ? 2 * hh + i : 4);
      qad[i] = L_Q + (q >> 2) * 1024 + ((q & 1) * 32 + r32) * 16 + ((q >> 1) & 1) * 8;
    }
    f32x16 oa, ob, z;
#pragma unroll
    for (int i = 0; i < 16; ++i) { oa[i] = 0.f; ob[i] = 0.f; z[i] = 0.f; }
    asm volatile("" : "+v"(oa), "+v"(ob), "+v"(kaddr), "+v"(taddr), "+v"(vaddr), "+v"(qad[0]), "+v"(qad[1]), "+v"(qad[2]));
    SB();
    if (role < 12) hstore();
    BAR();
    u32x4 q0u[2]; u32x2 q1[2];
    float bound[2], gtok[2];
    const float km = *(LAS const float*)(smem + L_Q + 10496);
#pragma unroll
    for (int xx = 0; xx < 2; ++xx) {
      u32x2 qa = *(LAS const u32x2*)(smem + qad[0] + xx * 5120), qb = *(LAS const u32x2*)(smem + qad[1] + xx * 5120),
            qc = *(LAS const u32x2*)(smem + qad[2] + xx * 5120);
      gtok[xx] = *(LAS const float*)(smem + L_Q + 10240 + (xx * 32 + r32) * 4);
      if (hh) { qc[0] = 0u; qc[1] = 0u; }
      q0u[xx] = u32x4{qa[0], qa[1], qb[0], qb[1]};
      q1[xx] = qc;
      float qq = sq2(qa[0], sq2(qa[1], sq2(qb[0], 0.f))) + sq2(qb[1], sq2(qc[0], sq2(qc[1], 0.f)));
      auto sw = __builtin_amdgcn_permlane32_swap(__float_as_uint(qq), __float_as_uint(qq), false, false);
      qq = __uint_as_float(sw[0]) + __uint_as_float(sw[1]);
      bound[xx] = __builtin_amdgcn_sqrtf(qq * km) * 1.001f + 1e-6f;
    }
    const bool fast = __all(fmaxf(bound[0], bound[1]) <= 40.f);
    float ma = 0.f, mb = 0.f;
    if (fast) {
      if (hh) {
#pragma unroll
        for (int xx = 0; xx < 2; ++xx) q1[xx][0] = cvtpk(-bound[xx], 0.f);
      }
      f32x16 sa, sb;
      bf16x8 k0, kt, v0, v1; u32x4 pa, pb = {0u, 0u, 0u, 0u};
      k0 = lds_b128(smem + kaddr); kt = lds_b128(smem + taddr);
      v0 = lds_b128(smem + vaddr); v1 = v0;
#define B4(x) __builtin_bit_cast(s16x4, x)
      sa = MFMA(k0, BC(q0u[0]), z); sa = MFMA8(LO4(kt), B4(q1[0]), sa);
#define E4(SC, i) SC[i] = EX2(SC[i]); SC[i + 1] = EX2(SC[i + 1]); SC[i + 2] = EX2(SC[i + 2]); SC[i + 3] = EX2(SC[i + 3])
#define UNIT(SC, SN, Q0N, KT, Q1N, OP, OC, L1, L2, L3, L4) do { \
      SN = MFMA(k0, BC(Q0N), z); SB(); L1; SB(); \
      E4(SC, 0); pa[0] = cvtpk(SC[0], SC[1]); pa[1] = cvtpk(SC[2], SC[3]); SB(); \
      SN = MFMA8(KT(kt), B4(Q1N), SN); SB(); L2; SB(); \
      E4(SC, 4); pa[2] = cvtpk(SC[4], SC[5]); pa[3] = cvtpk(SC[6], SC[7]); SB(); \
      OP = MFMA(v1, BC(pb), OP); SB(); L3; SB(); \
      E4(SC, 8); pb[0] = cvtpk(SC[8], SC[9]); pb[1] = cvtpk(SC[10], SC[11]); SB(); \
      OC = MFMA(v0, BC(pa), OC); SB(); L4; SB(); \
      E4(SC, 12); pb[2] = cvtpk(SC[12], SC[13]); pb[3] = cvtpk(SC[14], SC[15]); SB(); \
    } while (0)
#define PAIR(KC, VC, KN, TN, VN, P0, P1) do { \
      __builtin_amdgcn_s_setprio(P0); \
      UNIT(sa, sb, q0u[1], LO4, q1[1], ob, oa, k0 = lds_b128((KC) + 1024), , v1 = lds_b128((VC) + 640), ); \
      UNIT(sb, sa, q0u[0], HI4, q1[0], oa, ob, , , , v0 = lds_b128((VC) + 1280)); \
      __builtin_amdgcn_s_setprio(P1); \
      UNIT(sa, sb, q0u[1], HI4, q1[1], ob, oa, k0 = lds_b128(KN), kt = lds_b128(TN), v1 = lds_b128((VC) + 1920), ); \
      UNIT(sb, sa, q0u[0], LO4, q1[0], oa, ob, , , , v0 = lds_b128(VN)); \
    } while (0)
      int cur = 0;
#pragma unroll 1
      for (int step = 0; step < 8; ++step) {
        if (step == 1) WAIT_VM(0);
        if (step > 0) asm volatile("s_waitcnt lgkmcnt(1)\n\ts_barrier" ::: "memory");
        const int nxt = cur == 2 * KV_SLOT ? 0 : cur + KV_SLOT;
        LAS const char* kc = smem + kaddr + cur; LAS const char* kn = smem + kaddr + nxt;
        LAS const char* tc = smem + taddr + (cur & tmask); LAS const char* tn = smem + taddr + (nxt & tmask);
        LAS const char* vc = smem + vaddr + (cur & vmask); LAS const char* vn = smem + vaddr + (nxt & vmask);
        PAIR(kc, vc, kc + 5120, tc + 5120, vc + 5120, 3, 2);
        PAIR(kc + 5120, vc + 5120, kn, tn, vn, 1, 0);
        cur = nxt;
      }
      ob = MFMA(v1, BC(pb), ob);
    } else {
      ma = -1e30f; mb = -1e30f;
      int cur = 0;
#pragma unroll 1
      for (int step = 0; step < 8; ++step) {
        if (step > 0) { WAIT_VM(0); BAR(); }
#pragma unroll 1
        for (int t = 0; t < 4; ++t) {
          const int po = (t >> 1) * 5120, to = t & 1;
          bf16x8 kt = lds_b128(smem + taddr + (cur & tmask) + po);
          bf16x8 kf = lds_b128(smem + kaddr + cur + po + to * 1024);
          bf16x8 v0 = lds_b128(smem + vaddr + (cur & vmask) + po + to * 1280), v1 = lds_b128(smem + vaddr + (cur & vmask) + po + to * 1280 + 640);
#pragma unroll
          for (int xx = 0; xx < 2; ++xx) {
            f32x16 s = MFMA(kf, BC(q0u[xx]), z);
            s = MFMA8(to ? HI4(kt) : LO4(kt), __builtin_bit_cast(s16x4, q1[xx]), s);
            float mx = s[0];
#pragma unroll
            for (int i = 1; i < 16; ++i) mx = fmaxf(mx, s[i]);
            mx = fmaxf(mx, __shfl_xor(mx, 32));
            float& mr = xx ? mb : ma; f32x16& o = xx ? ob : oa;
            const float mn = fmaxf(mr, mx), al = EX2(mr - mn);
            mr = mn;
#pragma unroll
            for (int i = 0; i < 16; ++i) { o[i] *= al; s[i] = EX2(s[i] - mn); }
            u32x4 p0 = {cvtpk(s[0], s[1]), cvtpk(s[2], s[3]), cvtpk(s[4], s[5]), cvtpk(s[6], s[7])};
            u32x4 p1 = {cvtpk(s[8], s[9]), cvtpk(s[10], s[11]), cvtpk(s[12], s[13]), cvtpk(s[14], s[15])};
            o = MFMA(v0, BC(p0), o);
            o = MFMA(v1, BC(p1), o);
          }
        }
        cur = cur == 2 * KV_SLOT ? 0 : cur + KV_SLOT;
      }
    }
    oa[15] = ma; ob[15] = mb;
    {
      LAS char* cb = smem + L_KV + kh * (2 * KV_SLOT) + (w & 3) * 4096 + lane * 16;
      const f32x16& og = kh ? oa : ob;
#pragma unroll
      for (int g = 0; g < 4; ++g) *(LAS f32x4*)(cb + g * 1024) = f32x4{og[4 * g], og[4 * g + 1], og[4 * g + 2], og[4 * g + 3]};
      asm volatile("s_waitcnt lgkmcnt(0)" ::: "memory");
      lds_set(smem + L_ONES + 32 + w * 4);
      lds_await(smem + L_ONES + 32 + (w ^ 4) * 4);
    }
    {
      f32x16 o = kh ? ob : oa;
      f32x4 u[4];
      LAS const char* cb = smem + L_KV + (1 - kh) * (2 * KV_SLOT) + (w & 3) * 4096 + lane * 16;
#pragma unroll
      for (int g = 0; g < 4; ++g) u[g] = *(LAS const f32x4*)(cb + g * 1024);
      const float m0 = o[15], m1 = u[3][3], mm = fmaxf(m0, m1), f0 = EX2(m0 - mm), f1 = EX2(m1 - mm);
#pragma unroll
      for (int i = 0; i < 15; ++i) o[i] = o[i] * f0 + u[i >> 2][i & 3] * f1;
      auto lw = __builtin_amdgcn_permlane32_swap(__float_as_uint(o[8]), __float_as_uint(o[8]), false, false);
      const float l = __uint_as_float(lw[1]);
      const float sc = (kh ? gtok[1] : gtok[0]) * __builtin_amdgcn_rcpf(l);
      const unsigned ob_ = (unsigned)WS_O + ((tb * 2 + kh) * 40 + hg * 5) * 1024;
      u32x4 om = {cvtpk(o[0] * sc, o[1] * sc), cvtpk(o[2] * sc, o[3] * sc), cvtpk(o[4] * sc, o[5] * sc), cvtpk(o[6] * sc, o[7] * sc)};
      __builtin_amdgcn_raw_buffer_store_b128(om, rs, ob_ + (w & 3) * 1024 + l16, 0, 16);
      if (hh == 0) st8(ws + ob_ + 4096 + ((w & 1) * 32 + r32) * 16 + ((w >> 1) & 1) * 8, cvtpk(o[8] * sc, o[9] * sc), cvtpk(o[10] * sc, o[11] * sc));
      WAIT_VM(0);
    }
    if (lane == 0) {
      const unsigned n = __hip_atomic_fetch_add((LAS unsigned*)(smem + L_ONES + 12), 1u, __ATOMIC_RELAXED, __HIP_MEMORY_SCOPE_WORKGROUP);
      if (n == 7u) __hip_atomic_store((gu32*)(ws + WS_OF + (tb * 8 + hg) * 64), 1u, RLX_AGENT);
    }
    if (!active) return;
    BAR();
  }
  BAR();
  {
    const int tok = tg * 96 + (tid >> 3);
    f32x4 a0, a1; red_read(smem, tid, a0, a1);
    u32x4 ov = {cvtpk(a0[0], a0[1]), cvtpk(a0[2], a0[3]), cvtpk(a1[0], a1[1]), cvtpk(a1[2], a1[3])};
    auto ro = __builtin_amdgcn_make_buffer_rsrc((void*)out, 0, S_ * D_ * 2, 0x00020000);
    __builtin_amdgcn_raw_buffer_store_b128(ov, ro, tok * 1280 + (cg * 64 + (tid & 7) * 8) * 2, 0, 16);
  }
}

extern "C" void kernel_launch(void* const* d_in, const int* in_sizes, int n_in, void* d_out, int out_size, void* d_ws, size_t ws_size,
                              hipStream_t stream) {
  (void)hipFuncSetAttribute((const void*)fused_kernel, hipFuncAttributeMaxDynamicSharedMemorySize, L_TOTAL);
  fused_kernel<<<256, 768, L_TOTAL, stream>>>((const char*)d_in[0], (const char*)d_in[1], (const char*)d_in[2], (const char*)d_in[3],
                                              (const char*)d_in[4], (const char*)d_in[6], (const char*)d_in[7], (const char*)d_in[8],
                                              (const char*)d_in[9], (char*)d_ws, (char*)d_out);
}
```
